```python
import torch

def _softmax(x, axis=-1):
    return torch.softmax(x, dim=axis)
def _gelu(x):
    return torch.nn.functional.gelu(x, approximate="tanh")
def _silu(x):
    return torch.nn.functional.silu(x)
def _sigmoid(x):
    return torch.sigmoid(x)
def _layernorm(x, axis=-1, eps=1e-5):
    m = x.mean(dim=axis, keepdim=True)
    v = x.var(dim=axis, keepdim=True, unbiased=False)
    return (x - m) / torch.sqrt(v + eps)
def _scatter_add(tgt, idx, src, axis=0):
    return tgt.clone().index_add_(axis, idx, src)
def _segment_sum(src, seg, n):
    out = torch.zeros((n,) + src.shape[1:], dtype=src.dtype)
    return out.index_add_(0, seg, src)
def _pad(x, pw):
    flat = tuple(v for pair in reversed(pw) for v in pair)
    return torch.nn.functional.pad(x, flat)

kernel_name = 'standalone__deep_cross_pnn__recsys'

def setup_inputs():

    _w = (lambda a: (lambda t: t.to(torch.bfloat16) if t.is_floating_point() else t)(torch.as_tensor(a)))
    B, F, D, V = 4096, 100, 64, min(1000, 100000)
    H = 4 * D
    return {
        "idx": _w((lambda lo, hi, *s: torch.randint(lo, hi, s))(0, V, B, F).to(torch.int64)),
        "table": _w((lambda *s: torch.randn(*s, dtype=torch.bfloat16) * 1.6)(V, D) * (D ** -0.5)),
        "w_p": _w((lambda *s: torch.randn(*s, dtype=torch.bfloat16) * 0.05)(F * F) * ((F * F) ** -0.5)),
        "w1": _w((lambda *s: torch.randn(*s, dtype=torch.bfloat16) * 1.6)(F * D, H) * ((F * D) ** -0.5)),
        "b1": _w((lambda *s: torch.randn(*s, dtype=torch.bfloat16) * 1.6)(H) * 0.01),
        "w2": _w((lambda *s: torch.randn(*s, dtype=torch.bfloat16) * 0.05)(H) * (H ** -0.5)),
        "b2": _w((lambda *s: torch.randn(*s, dtype=torch.bfloat16) * 0.05)(1) * 0.01),
    }

def reference(idx, table, w_p, w1, b1, w2, b2):
    emb = table[idx]
    B = emb.shape[0]; F = emb.shape[1]; D = emb.shape[2]
    ip = torch.einsum('bid,bjd->bij', emb, emb)
    ip = ip.reshape(B, F * F)
    logit_p = ip @ w_p
    z = emb.reshape(B, F * D)
    h = _gelu(z @ w1 + b1)
    logit_d = h @ w2 + b2
    return logit_p + logit_d
```

```cpp
#include <hip/hip_runtime.h>
#include <hip/hip_bf16.h>
#include <stdint.h>
#include <utility>
#include <type_traits>


typedef __attribute__((ext_vector_type(8))) short bf16x8;
typedef __attribute__((ext_vector_type(16))) float f32x16;
typedef __attribute__((ext_vector_type(4))) float f32x4;
typedef __attribute__((ext_vector_type(4))) unsigned u32x4;

constexpr int NB = 4096, NF = 100, ND = 64, NV = 1000, NH = 256;
constexpr int NTILE = 28;

constexpr size_t WS_WT   = 0;
constexpr size_t WS_WSYM = WS_WT + (size_t)NF * ND * NH * 2;
constexpr size_t WS_GRAN = WS_WSYM + (size_t)NTILE * 64 * 16;
constexpr int NZB = 9 * NB * 8 / 16 / 256;

constexpr int L_ZROW = NV * 128;
constexpr int L_IDX  = L_ZROW + 128;
constexpr int L_BYTES = L_IDX + 112 * 65 * 4;

#define SB() __builtin_amdgcn_sched_barrier(0)
__device__ __forceinline__ float bf2f(unsigned short u) { return __uint_as_float(((unsigned)u) << 16); }
__device__ __forceinline__ float bfr(float x) { return __bfloat162float(__float2bfloat16(x)); }

__global__ __launch_bounds__(256) void prep_kernel(const unsigned short* __restrict__ w1,
                                                   const unsigned short* __restrict__ wp,
                                                   u32x4* __restrict__ Wt, float* __restrict__ Wsym,
                                                   u32x4* __restrict__ gran) {
  const int bid = blockIdx.x, tid = threadIdx.x;
  if (bid >= 400 + NTILE) { gran[(bid - 400 - NTILE) * 256 + tid] = u32x4{0u, 0u, 0u, 0u}; return; }
  if (bid < 400) {
    const int o2 = bid * 256 + tid;
    const int o = o2 * 2;
    const int l = o & 63, j = (o >> 6) & 1, s = (o >> 7) & 3;
    const int ff = o >> 9, f = ff % NF, ns = ff / NF;
    const int k0 = f * 64 + s * 16 + (l >> 5) * 8;
    const int n = ns * 64 + j * 32 + (l & 31);
    const unsigned* p = (const unsigned*)(w1 + (size_t)k0 * NH + n);
    unsigned e[8];
#pragma unroll
    for (int i = 0; i < 8; ++i) e[i] = p[i * (NH / 2)];
    u32x4 r0, r1;
#pragma unroll
    for (int d = 0; d < 4; ++d) {
      r0[d] = __builtin_amdgcn_perm(e[2 * d + 1], e[2 * d], 0x05040100u);
      r1[d] = __builtin_amdgcn_perm(e[2 * d + 1], e[2 * d], 0x07060302u);
    }
    Wt[o] = r0; Wt[o + 1] = r1;
  } else {
    const int t = bid - 400;
    int ti = 0, rem = t;
    while (rem >= 7 - ti) { rem -= 7 - ti; ++ti; }
    const int tj = ti + rem;
    const int l = tid >> 2, r = tid & 3;
    const int I = 16 * ti + 4 * (l >> 4) + r, J = 16 * tj + (l & 15);
    float w = 0.f;
    if (I < NF && J < NF) {
      w = bf2f(wp[I * NF + J]);
      if (ti != tj) w += bf2f(wp[J * NF + I]);
    }
    Wsym[t * 256 + tid] = w;
  }
}

template <int B, class Fn, int... Is>
__device__ __forceinline__ void static_for_impl(Fn&& fn, std::integer_sequence<int, Is...>) {
  (fn(std::integral_constant<int, B + Is>{}), ...);
}
template <int B, int E, class Fn>
__device__ __forceinline__ void static_for(Fn&& fn) {
  if constexpr (E > B) static_for_impl<B>(fn, std::make_integer_sequence<int, E - B>{});
}
__device__ __forceinline__ void mfma32(f32x16& c, const bf16x8& a, const bf16x8& b) {
  asm volatile("v_mfma_f32_32x32x16_bf16 %0, %1, %2, %0" : "+a"(c) : "a"(a), "a"(b));
}
template <int FIRST> __device__ __forceinline__ void mfma32f(f32x16& c, const bf16x8& a, const bf16x8& b) {
  if constexpr (FIRST) asm volatile("v_mfma_f32_32x32x16_bf16 %0, %1, %2, 0" : "=&a"(c) : "a"(a), "a"(b));
  else mfma32(c, a, b);
}
template <int AG> __device__ __forceinline__ f32x4 mfma16_0(const bf16x8& a, const bf16x8& b) {
  f32x4 d;
  if constexpr (AG) asm volatile("v_mfma_f32_16x16x32_bf16 %0, %1, %2, 0" : "=&v"(d) : "a"(a), "a"(b));
  else              asm volatile("v_mfma_f32_16x16x32_bf16 %0, %1, %2, 0" : "=&v"(d) : "v"(a), "v"(b));
  return d;
}
template <int AG> __device__ __forceinline__ void mfma16(f32x4& c, const bf16x8& a, const bf16x8& b) {
  if constexpr (AG) asm volatile("v_mfma_f32_16x16x32_bf16 %0, %1, %2, %0" : "+v"(c) : "a"(a), "a"(b));
  else              asm volatile("v_mfma_f32_16x16x32_bf16 %0, %1, %2, %0" : "+v"(c) : "v"(a), "v"(b));
}
__device__ constexpr int tile_i(int t) { int ti = 0; while (t >= 7 - ti) { t -= 7 - ti; ++ti; } return ti; }
__device__ constexpr int tile_j(int t) { int ti = 0; while (t >= 7 - ti) { t -= 7 - ti; ++ti; } return ti + t; }

__global__ __launch_bounds__(256, 1) void main_kernel(const int* __restrict__ idx,
                                                      const u32x4* __restrict__ table,
                                                      const bf16x8* __restrict__ Wt,
                                                      const f32x4* __restrict__ Wsym,
                                                      const unsigned short* __restrict__ b1,
                                                      const unsigned short* __restrict__ w2,
                                                      const unsigned short* __restrict__ b2,
                                                      unsigned long long* gran,
                                                      __hip_bfloat16* __restrict__ out
                                                      ) {
  extern __shared__ __attribute__((aligned(16))) char lds[];
  const int tid = threadIdx.x, lane = tid & 63;
  const int wave = __builtin_amdgcn_readfirstlane(tid >> 6);
  const int bid = blockIdx.x;
  const int xx = bid & 7, ns = xx & 3, mt = (bid >> 3) * 2 + (xx >> 2);
  const int b0 = mt * 64;

  const int f0 = wave * 25;
  const __amdgpu_buffer_rsrc_t wrs = __builtin_amdgcn_make_buffer_rsrc(
      (void*)(Wt + ((size_t)(ns * NF + f0) * 8) * 64), 0, 25 * 8 * 1024, 0x00020000);
  const int loff = lane * 16;
#define WLOAD(q) __builtin_bit_cast(bf16x8, __builtin_amdgcn_raw_buffer_load_b128(wrs, loff, (q) * 1024, 0))

  f32x4 wv[NTILE];
  bf16x8 Bf[2][8];
  {
    typedef __attribute__((ext_vector_type(4))) int i32x4u;
    const int* ip = idx + (b0 + (tid >> 2)) * NF + (tid & 3) * 25;
    i32x4u iq[6];
    int il;
    asm volatile("global_load_dwordx4 %0, %7, off\n\tglobal_load_dwordx4 %1, %7, off offset:16\n\t"
                 "global_load_dwordx4 %2, %7, off offset:32\n\tglobal_load_dwordx4 %3, %7, off offset:48\n\t"
                 "global_load_dwordx4 %4, %7, off offset:64\n\tglobal_load_dwordx4 %5, %7, off offset:80\n\t"
                 "global_load_dword %6, %7, off offset:96"
                 : "=&v"(iq[0]), "=&v"(iq[1]), "=&v"(iq[2]), "=&v"(iq[3]), "=&v"(iq[4]), "=&v"(iq[5]), "=&v"(il)
                 : "v"(ip) : "memory");
    SB();
    auto dma = [&](int i) {
      const int piece = wave + i * 4;
      if (piece < 125) {
        const int p = piece * 64 + lane, v = p >> 3, c = (p & 7) ^ ((v >> 1) & 7);
        __builtin_amdgcn_global_load_lds((const unsigned*)(table + v * 8 + c), (unsigned*)(lds + piece * 1024), 16, 0, 0);
      }
    };
#pragma unroll
    for (int i = 0; i < 18; ++i) dma(i);
    SB();
    asm volatile("s_waitcnt vmcnt(17)"
                 : "+v"(iq[0]), "+v"(iq[1]), "+v"(iq[2]), "+v"(iq[3]), "+v"(iq[4]), "+v"(iq[5]), "+v"(il) :: "memory");
    SB();
    char* ib = lds + L_IDX + ((tid & 3) * 25 * 65 + (tid >> 2)) * 4;
#pragma unroll
    for (int i = 0; i < 25; ++i) {
      int v = i < 24 ? iq[i >> 2][i & 3] : il;
      v = v < 0 ? 0 : (v >= NV ? NV - 1 : v);
      *(unsigned*)(ib + i * 260) = (unsigned)((v << 7) | ((v << 3) & 0x70));
      if (i >= 4 && i < 18) dma(14 + i);
    }
#pragma unroll
    for (int q = 0; q < 8; ++q) Bf[0][q] = WLOAD(q);
#pragma unroll
    for (int i = 0; i < 3; ++i) {
      const int e = tid + i * 256;
      const int f = NF + (e >> 6), row = e & 63;
      *(unsigned*)(lds + L_IDX + (f * 65 + row) * 4) = (unsigned)L_ZROW;
    }
    if (tid < 8) *(u32x4*)(lds + L_ZROW + tid * 16) = u32x4{0u, 0u, 0u, 0u};
    asm volatile("s_waitcnt vmcnt(0)" ::: "memory");
  }
  __syncthreads();
#pragma unroll
  for (int q = 0; q < 8; ++q) Bf[1][q] = WLOAD(8 + q);
#pragma unroll
  for (int t = 0; t < 6; ++t) wv[t] = Wsym[t * 64 + lane];

  f32x16 acc[2][2];
  {
    const int r32 = lane & 31;
    const unsigned hx = (lane >> 5) << 4;
    const char* ia = lds + L_IDX + (f0 * 65 + r32) * 4;
    const int r16 = lane & 15;
    const unsigned q16 = (lane >> 4) << 4;
    const char* pa = lds + L_IDX + (r16 * 65 + ns * 16 + wave * 4) * 4;

    bf16x8 Af[2][8];
    unsigned xa[2][2];
    bf16x8 E[2][14];
    unsigned xr[7];
    f32x4 g[4];
    float sp[4][4];
#pragma unroll
    for (int e = 0; e < 4; ++e) { sp[e][0] = 0.f; sp[e][1] = 0.f; sp[e][2] = 0.f; sp[e][3] = 0.f; }
    {
      const unsigned x0 = *(const unsigned*)(ia), x1 = *(const unsigned*)(ia + 128);
#pragma unroll
      for (int t = 0; t < 7; ++t) xr[t] = *(const unsigned*)(pa + t * 16 * 65 * 4) ^ q16;
#pragma unroll
      for (int s = 0; s < 4; ++s) {
        Af[0][s * 2 + 0] = *(const bf16x8*)(lds + (x0 ^ (hx + (s << 5))));
        Af[0][s * 2 + 1] = *(const bf16x8*)(lds + (x1 ^ (hx + (s << 5))));
      }
      xa[1][0] = *(const unsigned*)(ia + 65 * 4); xa[1][1] = *(const unsigned*)(ia + 65 * 4 + 128);
#pragma unroll
      for (int t = 0; t < 7; ++t) {
        E[0][2 * t] = *(const bf16x8*)(lds + xr[t]);
        E[0][2 * t + 1] = *(const bf16x8*)(lds + (xr[t] ^ 64));
      }
    }
    SB();
    auto pnn_mfma = [&](auto T_, auto H_) {
      constexpr int T = T_, H = H_;
      constexpr int e = T / NTILE, t = T % NTILE, pc = e & 1, pn = pc ^ 1;
      constexpr int ti = tile_i(t), tj = tile_j(t);
      if constexpr (H == 0) g[T % 4] = mfma16_0<pn>(E[pc][2 * ti], E[pc][2 * tj]);
      else mfma16<pn>(g[T % 4], E[pc][2 * ti + 1], E[pc][2 * tj + 1]);
      SB();
    };
    auto pnn_fma = [&](auto T_, auto H_) {
      constexpr int T = T_, H = H_;
      if constexpr (T >= 2) {
        constexpr int U = T - 2, ue = U / NTILE, ut = U % NTILE;
        sp[ue][2 * H] = fmaf(g[U % 4][2 * H], wv[ut][2 * H], sp[ue][2 * H]);
        sp[ue][2 * H + 1] = fmaf(g[U % 4][2 * H + 1], wv[ut][2 * H + 1], sp[ue][2 * H + 1]);
      }
    };
    auto pnn_pref = [&](auto T_) {
      constexpr int T = T_;
      constexpr int e = T / NTILE, t = T % NTILE, pn = (e & 1) ^ 1;
      if constexpr (e < 3) {
        if constexpr (t < 2) {
#pragma unroll
          for (int k = t * 4; k < (t * 4 + 4 < 7 ? t * 4 + 4 : 7); ++k)
            xr[k] = *(const unsigned*)(pa + k * 16 * 65 * 4 + (e + 1) * 4) ^ q16;
        } else if constexpr (t >= 4 && t < 18) {
          constexpr int k = t - 4;
          E[pn][k] = *(const bf16x8*)(lds + (xr[k >> 1] ^ ((k & 1) << 6)));
        }
      }
    };
    static_for<0, 100>([&](auto I_) {
      constexpr int i = I_;
      constexpr int f = i >> 2, s = i & 3;
      constexpr int cur = f & 1, nxt = cur ^ 1;
      constexpr int T0 = (i * 112) / 100, T1 = ((i + 1) * 112) / 100;
      constexpr bool two = T1 - T0 == 2;
      using C0 = std::integral_constant<int, 0>; using C1 = std::integral_constant<int, 1>;
      using TA = std::integral_constant<int, T0>; using TB = std::integral_constant<int, T0 + 1>;
      mfma32f<i == 0>(acc[0][0], Bf[cur][s * 2 + 0], Af[cur][s * 2 + 0]);
      SB();
      if (f + 1 < 25) Af[nxt][s * 2 + 0] = *(const bf16x8*)(lds + (xa[nxt][0] ^ (hx + (s << 5))));
      if (s == 1 && f + 2 < 25) xa[cur][0] = *(const unsigned*)(ia + (f + 2) * 65 * 4);
      pnn_fma(TA{}, C0{});
      SB();
      pnn_mfma(TA{}, C0{});
      mfma32f<i == 0>(acc[1][0], Bf[cur][s * 2 + 0], Af[cur][s * 2 + 1]);
      SB();
      if (f + 1 < 25) Af[nxt][s * 2 + 1] = *(const bf16x8*)(lds + (xa[nxt][1] ^ (hx + (s << 5))));
      if (s == 1 && f + 2 < 25) xa[cur][1] = *(const unsigned*)(ia + (f + 2) * 65 * 4 + 128);
      pnn_fma(TA{}, C1{});
      SB();
      if constexpr (two) pnn_mfma(TB{}, C0{});
      mfma32f<i == 0>(acc[0][1], Bf[cur][s * 2 + 1], Af[cur][s * 2 + 0]);
      SB();
      if (f + 2 < 25) Bf[cur][s * 2 + 0] = WLOAD((f + 2) * 8 + s * 2 + 0);
      if constexpr (i + 6 < NTILE) wv[i + 6] = Wsym[(i + 6) * 64 + lane];
      if constexpr (two) pnn_fma(TB{}, C0{});
      SB();
      pnn_mfma(TA{}, C1{});
      mfma32f<i == 0>(acc[1][1], Bf[cur][s * 2 + 1], Af[cur][s * 2 + 1]);
      SB();
      if (f + 2 < 25) Bf[cur][s * 2 + 1] = WLOAD((f + 2) * 8 + s * 2 + 1);
      pnn_pref(TA{});
      if constexpr (two) { pnn_fma(TB{}, C1{}); pnn_pref(TB{}); }
      SB();
      if constexpr (two) pnn_mfma(TB{}, C1{});
    });
    asm volatile("s_nop 15\n\ts_nop 15" ::: "memory");
    SB();
#pragma unroll
    for (int U = 110; U < 112; ++U) {
      const int ut = U % NTILE;
      sp[3][0] = fmaf(g[U % 4][0], wv[ut][0], sp[3][0]); sp[3][1] = fmaf(g[U % 4][1], wv[ut][1], sp[3][1]);
      sp[3][2] = fmaf(g[U % 4][2], wv[ut][2], sp[3][2]); sp[3][3] = fmaf(g[U % 4][3], wv[ut][3], sp[3][3]);
    }
    {
      float sum[4];
#pragma unroll
      for (int e = 0; e < 4; ++e) sum[e] = (sp[e][0] + sp[e][1]) + (sp[e][2] + sp[e][3]);
      const bool c5 = lane & 32, c4 = lane & 16;
      float u2[2], u1;
#pragma unroll
      for (int i = 0; i < 2; ++i) {
        const float send = c5 ? sum[i] : sum[i + 2], keep = c5 ? sum[i + 2] : sum[i];
        u2[i] = keep + __shfl_xor(send, 32, 64);
      }
      {
        const float send = c4 ? u2[0] : u2[1], keep = c4 ? u2[1] : u2[0];
        u1 = keep + __shfl_xor(send, 16, 64);
      }
#pragma unroll
      for (int m = 8; m >= 1; m >>= 1) u1 += __shfl_xor(u1, m, 64);
      if ((lane & 15) == 0)
        __hip_atomic_store(gran + 8 * NB + b0 + ns * 16 + wave * 4 + (lane >> 4),
                           (1ull << 32) | __float_as_uint(u1), __ATOMIC_RELAXED, __HIP_MEMORY_SCOPE_AGENT);
    }
  }
  typedef __attribute__((ext_vector_type(2))) unsigned u32x2;
  u32x2 bq[4], wq[4];
  {
    const int nb = ns * 64 + (wave & 1) * 32 + 4 * (lane >> 5);
#pragma unroll
    for (int g = 0; g < 4; ++g) {
      bq[g] = *(const u32x2*)(b1 + nb + 8 * g);
      wq[g] = *(const u32x2*)(w2 + nb + 8 * g);
    }
  }
  __syncthreads();
#pragma unroll
  for (int a = 0; a < 2; ++a)
#pragma unroll
    for (int b = 0; b < 2; ++b)
      if (a * 2 + b != wave) {
#pragma unroll
        for (int g = 0; g < 4; ++g) {
          f32x4 v = {acc[a][b][4 * g], acc[a][b][4 * g + 1], acc[a][b][4 * g + 2], acc[a][b][4 * g + 3]};
          *(f32x4*)(lds + ((((wave * 4 + a * 2 + b) * 4 + g) * 64 + lane) << 4)) = v;
        }
      }
  f32x16 own = wave == 0 ? acc[0][0] : wave == 1 ? acc[0][1] : wave == 2 ? acc[1][0] : acc[1][1];
  __syncthreads();
  {
    const int t = wave;
    float x0 = 0.f, x1 = 0.f;
#pragma unroll
    for (int g = 0; g < 4; ++g) {
      f32x4 s = {own[4 * g], own[4 * g + 1], own[4 * g + 2], own[4 * g + 3]};
#pragma unroll
      for (int w = 1; w < 4; ++w) s += *(const f32x4*)(lds + ((((((w + t) & 3) * 4 + t) * 4 + g) * 64 + lane) << 4));
#pragma unroll
      for (int i = 0; i < 4; ++i) {
        const unsigned bw = bq[g][i >> 1], ws_ = wq[g][i >> 1];
        const float bb = __uint_as_float((i & 1) ? (bw & 0xffff0000u) : (bw << 16));
        const float ww = __uint_as_float((i & 1) ? (ws_ & 0xffff0000u) : (ws_ << 16));
        const float x = bfr(bfr(s[i]) + bb);
        const float u = x * (1.5957691216057308f + 0.0713548162726009f * x * x);
        const float sg = __builtin_amdgcn_rcpf(1.f + __expf(-u));
        const float hv = bfr(x * sg) * ww;
        if (i & 1) x1 += hv; else x0 += hv;
      }
    }
    float x = x0 + x1;
    x += __shfl_xor(x, 32, 64);
    if (lane < 32)
      __hip_atomic_store(gran + (ns * 2 + (t & 1)) * NB + b0 + (t >> 1) * 32 + lane,
                         (1ull << 32) | __float_as_uint(x), __ATOMIC_RELAXED, __HIP_MEMORY_SCOPE_AGENT);
  }
  if (ns == (mt & 3) && wave == 0) {
    const int b = b0 + lane;
    unsigned long long gv[9];
    for (int spin = 0; spin < (1 << 22); ++spin) {
      bool ok = true;
#pragma unroll
      for (int j = 0; j < 9; ++j) {
        gv[j] = __hip_atomic_load(gran + j * NB + b, __ATOMIC_RELAXED, __HIP_MEMORY_SCOPE_AGENT);
        ok = ok && (gv[j] >> 32) == 1ull;
      }
      if (__all(ok)) break;
      __builtin_amdgcn_s_sleep(1);
    }
    float pv[9];
#pragma unroll
    for (int j = 0; j < 9; ++j) pv[j] = __uint_as_float((unsigned)gv[j]);
    const float d = ((pv[0] + pv[1]) + (pv[2] + pv[3])) + ((pv[4] + pv[5]) + (pv[6] + pv[7]));
    const float ld = bfr(bfr(d) + bf2f(b2[0]));
    out[b] = __float2bfloat16(bfr(pv[8]) + ld);
  }
}

extern "C" void kernel_launch(void* const* d_in, const int* in_sizes, int n_in,
                              void* d_out, int out_size, void* d_ws, size_t ws_size,
                              hipStream_t stream) {
  const int* idx = (const int*)d_in[0];
  const u32x4* table = (const u32x4*)d_in[1];
  const unsigned short* wp = (const unsigned short*)d_in[2];
  const unsigned short* w1 = (const unsigned short*)d_in[3];
  const unsigned short* b1 = (const unsigned short*)d_in[4];
  const unsigned short* w2 = (const unsigned short*)d_in[5];
  const unsigned short* b2 = (const unsigned short*)d_in[6];
  char* ws = (char*)d_ws;
  u32x4* Wt = (u32x4*)(ws + WS_WT);
  float* Wsym = (float*)(ws + WS_WSYM);
  unsigned long long* gran = (unsigned long long*)(ws + WS_GRAN);

  (void)hipFuncSetAttribute((const void*)main_kernel, hipFuncAttributeMaxDynamicSharedMemorySize, L_BYTES);
  prep_kernel<<<400 + NTILE + NZB, 256, 0, stream>>>(w1, wp, Wt, Wsym, (u32x4*)gran);
  main_kernel<<<256, 256, L_BYTES, stream>>>(idx, table, (const bf16x8*)Wt, (const f32x4*)Wsym, b1, w2, b2, gran, (__hip_bfloat16*)d_out);
}
```
